```python
import torch

def _softmax(x, axis=-1):
    return torch.softmax(x, dim=axis)

def _gelu(x):
    return torch.nn.functional.gelu(x, approximate='tanh')

def _silu(x):
    return torch.nn.functional.silu(x)

def _sigmoid(x):
    return torch.sigmoid(x)

def _layernorm(x, axis=-1, eps=1e-05):
    m = x.mean(dim=axis, keepdim=True)
    v = x.var(dim=axis, keepdim=True, unbiased=False)
    return (x - m) / torch.sqrt(v + eps)

def _scatter_add(tgt, idx, src, axis=0):
    return tgt.clone().index_add_(axis, idx, src)

def _segment_sum(src, seg, n):
    out = torch.zeros((n,) + src.shape[1:], dtype=src.dtype)
    return out.index_add_(0, seg, src)

def _pad(x, pw):
    flat = tuple((v for pair in reversed(pw) for v in pair))
    return torch.nn.functional.pad(x, flat)
kernel_name = 'adaLN_cond__mha_block_cached__glu_gated_geglu__decode'

def setup_inputs():

    _w = lambda a: (lambda t: t.to(torch.bfloat16) if t.is_floating_point() else t)(torch.as_tensor(a))

    def _A_setup():
        B, Sq, D = (1, 1, 512)
        H, Sk, Dh = (32, 65536, 128)
        return {'x': _w((lambda *s: torch.randn(*s, dtype=torch.bfloat16) * 0.05)(B, Sq, D)), 'k_cache': _w((lambda *s: torch.randn(*s, dtype=torch.bfloat16) * 0.8)(B, H, Sk, Dh)), 'v_cache': _w((lambda *s: torch.randn(*s, dtype=torch.bfloat16) * 3.2)(B, H, Sk, Dh)), 'wq': _w((lambda *s: torch.randn(*s, dtype=torch.bfloat16) * 0.8)(D, H * Dh) * D ** (-0.5)), 'wo': _w((lambda *s: torch.randn(*s, dtype=torch.bfloat16) * 3.2)(H * Dh, D) * (H * Dh) ** (-0.5))}
    _A_in = _A_setup()

    def _B_setup():
        B, S, Dm, Df = (1, 1, 512, 1792)
        return {'x': _w((lambda *s: torch.randn(*s, dtype=torch.bfloat16) * 0.05)(B, S, Dm)), 'w_gate': _w((lambda *s: torch.randn(*s, dtype=torch.bfloat16) * 3.2)(Dm, Df) * Dm ** (-0.5)), 'w_up': _w((lambda *s: torch.randn(*s, dtype=torch.bfloat16) * 3.2)(Dm, Df) * Dm ** (-0.5)), 'w_down': _w((lambda *s: torch.randn(*s, dtype=torch.bfloat16) * 3.2)(Df, Dm) * Df ** (-0.5))}
    _B_in = _B_setup()
    return {'A_x': _A_in['x'], 'A_k_cache': _A_in['k_cache'], 'A_v_cache': _A_in['v_cache'], 'A_wq': _A_in['wq'], 'A_wo': _A_in['wo'], 'B_x': _B_in['x'], 'B_w_gate': _B_in['w_gate'], 'B_w_up': _B_in['w_up'], 'B_w_down': _B_in['w_down'], 'c': _w((lambda *s: torch.randn(*s, dtype=torch.bfloat16) * 0.4)(1, 512) * 8.0), 'w_mod': _w((lambda *s: torch.randn(*s, dtype=torch.bfloat16) * 0.4)(512, 6 * 512) * 512 ** (-0.5) * 8.0)}

def reference(A_x, A_k_cache, A_v_cache, A_wq, A_wo, B_x, B_w_gate, B_w_up, B_w_down, c, w_mod):

    def _A_ref(x, k_cache, v_cache, wq, wo):
        B, Sq, D = x.shape
        H, Dh = (32, 128)
        q = (lambda x, *d: x.permute(*d))((x @ wq).reshape(B, Sq, H, Dh), 0, 2, 1, 3)
        scores = torch.einsum('bhqd,bhkd->bhqk', q, k_cache) * Dh ** (-0.5)
        p = _softmax(scores, axis=-1)
        o = torch.einsum('bhqk,bhkd->bhqd', p, v_cache)
        return (lambda x, *d: x.permute(*d))(o, 0, 2, 1, 3).reshape(B, Sq, H * Dh) @ wo

    def _B_ref(x, w_gate, w_up, w_down):
        g = torch.einsum('bsd,df->bsf', x, w_gate)
        u = torch.einsum('bsd,df->bsf', x, w_up)
        h = _gelu(g) * u
        return torch.einsum('bsf,fd->bsd', h, w_down)
    x = A_x
    mod = (_silu(c) @ w_mod).reshape(c.shape[0], 6, 1, -1)
    sa, ca, ga, sb, cb, gb = (mod[:, 0].clone(), mod[:, 1].clone(), mod[:, 2].clone(), mod[:, 3].clone(), mod[:, 4].clone(), mod[:, 5].clone())
    h = _layernorm(x, axis=-1) * (1 + ca) + sa
    x = x + ga * _A_ref(h, A_k_cache, A_v_cache, A_wq, A_wo)
    h = _layernorm(x, axis=-1) * (1 + cb) + sb
    x = x + gb * _B_ref(h, B_w_gate, B_w_up, B_w_down)
    return x
```

```cpp
#include <hip/hip_runtime.h>
#include <hip/hip_bf16.h>
#include <stdint.h>

typedef unsigned short u16;
typedef unsigned u32x4 __attribute__((ext_vector_type(4)));
typedef float f32x4 __attribute__((ext_vector_type(4)));
typedef __bf16 bf16x2 __attribute__((ext_vector_type(2)));

constexpr int D = 512, H = 32, SK = 65536, DH = 128, DF = 1792;
constexpr int NSPLIT = 8;

__device__ __forceinline__ float bf2f(u16 v) { return __uint_as_float((unsigned)v << 16); }
__device__ __forceinline__ float rbf(float f) { return __bfloat162float(__float2bfloat16(f)); }
__device__ __forceinline__ u16 f2bf(float f) { __hip_bfloat16 b = __float2bfloat16(f); return *reinterpret_cast<u16*>(&b); }
__device__ __forceinline__ float lo16(unsigned w) { return __uint_as_float(w << 16); }
__device__ __forceinline__ float hi16(unsigned w) { return __uint_as_float(w & 0xffff0000u); }
__device__ __forceinline__ float dot2(unsigned a, unsigned b, float acc) {
  return __builtin_amdgcn_fdot2_f32_bf16(__builtin_bit_cast(bf16x2, a), __builtin_bit_cast(bf16x2, b), acc, false);
}
template <int CTRL> __device__ __forceinline__ float dpp(float x) {
  return __builtin_bit_cast(float, __builtin_amdgcn_mov_dpp(__builtin_bit_cast(int, x), CTRL, 0xf, 0xf, true));
}
__device__ __forceinline__ float wave_sum(float v) {
#pragma unroll
  for (int m = 32; m >= 1; m >>= 1) v += __shfl_xor(v, m);
  return v;
}

template <int KN>
__device__ __forceinline__ void gemv64(const u16* __restrict__ W, int N, int k0, int col0, const float* sh_in, float* sh_red, float* sh_out) {
  const int tid = threadIdx.x, cg = tid & 7, rg = tid >> 3;
  float acc[8];
#pragma unroll
  for (int j = 0; j < 8; ++j) acc[j] = 0.f;
#pragma unroll 4
  for (int r = rg; r < KN; r += 32) {
    const u32x4 w = *reinterpret_cast<const u32x4*>(W + (size_t)(k0 + r) * N + col0 + cg * 8);
    const float a = sh_in[r];
#pragma unroll
    for (int j = 0; j < 4; ++j) { acc[2 * j] = fmaf(a, lo16(w[j]), acc[2 * j]); acc[2 * j + 1] = fmaf(a, hi16(w[j]), acc[2 * j + 1]); }
  }
#pragma unroll
  for (int j = 0; j < 8; ++j) sh_red[rg * 64 + cg * 8 + j] = acc[j];
  __syncthreads();
  if (tid < 64) { float s = 0.f;
#pragma unroll
    for (int g = 0; g < 32; ++g) s += sh_red[g * 64 + tid];
    sh_out[tid] = s; }
  __syncthreads();
}

__device__ __forceinline__ void adaln(const float* xs, const u16* shift, const u16* scale, float* hs, float* red) {
  const int tid = threadIdx.x;
  float a = xs[tid], b = xs[tid + 256];
  float s = wave_sum(a + b);
  if ((tid & 63) == 0) red[tid >> 6] = s;
  __syncthreads();
  const float mean = (red[0] + red[1] + red[2] + red[3]) * (1.f / D);
  __syncthreads();
  float da = a - mean, db = b - mean;
  float v = wave_sum(da * da + db * db);
  if ((tid & 63) == 0) red[tid >> 6] = v;
  __syncthreads();
  const float var = (red[0] + red[1] + red[2] + red[3]) * (1.f / D);
  const float mb = rbf(mean), vb = rbf(var);
  const float den = rbf(sqrtf(rbf(vb + 1e-5f)));
#pragma unroll
  for (int i = 0; i < 2; ++i) { const int e = tid + 256 * i; const float xv = i ? b : a;
    const float n = rbf(rbf(xv - mb) / den);
    const float sc = rbf(1.f + bf2f(scale[e]));
    hs[e] = rbf(rbf(n * sc) + bf2f(shift[e])); }
  __syncthreads();
}

__global__ void __launch_bounds__(256) k_mod(const u16* __restrict__ c, const u16* __restrict__ wmod, u16* __restrict__ mod) {
  __shared__ float sin_[D]; __shared__ float red[32 * 64]; __shared__ float out[64];
  for (int i = threadIdx.x; i < D; i += 256) { const float v = bf2f(c[i]); sin_[i] = rbf(v / (1.f + __expf(-v))); }
  __syncthreads();
  gemv64<D>(wmod, 6 * D, 0, blockIdx.x * 64, sin_, red, out);
  if (threadIdx.x < 64) mod[blockIdx.x * 64 + threadIdx.x] = f2bf(out[threadIdx.x]);
}

__global__ void __launch_bounds__(256) k_q(const u16* __restrict__ x, const u16* __restrict__ mod, const u16* __restrict__ wq, u16* __restrict__ q) {
  __shared__ float xs[D]; __shared__ float hs[D]; __shared__ float red[32 * 64]; __shared__ float out[64];
  for (int i = threadIdx.x; i < D; i += 256) xs[i] = bf2f(x[i]);
  __syncthreads();
  adaln(xs, mod, mod + D, hs, red);
  gemv64<D>(wq, H * DH, 0, blockIdx.x * 64, hs, red, out);
  if (threadIdx.x < 64) q[blockIdx.x * 64 + threadIdx.x] = f2bf(out[threadIdx.x]);
}

constexpr int AW = 8;
constexpr int KEYS_PER_WG = SK / NSPLIT;
constexpr int NIT = KEYS_PER_WG / (64 * AW);
constexpr int XOR1 = 0xB1, XOR2 = 0x4E, XOR7 = 0x141, XOR8 = 0x128, ROR4 = 0x124;

__global__ void __launch_bounds__(AW * 64) k_attn(const u16* __restrict__ q, const u16* __restrict__ K, const u16* __restrict__ V, float* __restrict__ part) {
  __shared__ float mrg[AW][132];
  const int head = blockIdx.x / NSPLIT, split = blockIdx.x % NSPLIT;
  const int tid = threadIdx.x, lane = tid & 63, wave = __builtin_amdgcn_readfirstlane(tid >> 6), r = lane >> 4, c = lane & 15;
  const u32x4 qv = *reinterpret_cast<const u32x4*>(q + head * DH + 8 * c);
  const size_t base = ((size_t)head * SK + (size_t)split * KEYS_PER_WG) * DH;
  const u32x4* kp = reinterpret_cast<const u32x4*>(K + base) + lane;
  const u32x4* vp = reinterpret_cast<const u32x4*>(V + base) + lane;
  float m = -INFINITY, l = 0.f, acc[8];
#pragma unroll
  for (int k = 0; k < 8; ++k) acc[k] = 0.f;
  constexpr float SCALE = 0.08838834764831845f, LOG2E = 1.4426950408889634f;
  for (int it = 0; it < NIT; ++it) {
    const size_t off = (size_t)(it * AW + wave) * 64 * 16;
    u32x4 kk[16], vv[16];
#pragma unroll
    for (int t = 0; t < 16; ++t) kk[t] = __builtin_nontemporal_load(kp + off + t * 64);
#pragma unroll
    for (int t = 0; t < 16; ++t) vv[t] = __builtin_nontemporal_load(vp + off + t * 64);
    float x[16];
#pragma unroll
    for (int t = 0; t < 16; ++t) { float d = 0.f;
#pragma unroll
      for (int k = 0; k < 4; ++k) d = dot2(kk[t][k], qv[k], d);
      x[t] = d; }
#define TR_STEP(HALF, CTRL, BIT) _Pragma("unroll") for (int h = 0; h < (HALF); ++h) { \
      const float lo_ = x[h], hi_ = x[h + (HALF)]; const float t1_ = lo_ + dpp<CTRL>(lo_), t2_ = hi_ + dpp<CTRL>(hi_); \
      x[h] = (c & (BIT)) ? t2_ : t1_; }
    TR_STEP(8, XOR8, 8) TR_STEP(4, XOR7, 4) TR_STEP(2, XOR2, 2) TR_STEP(1, XOR1, 1)
#undef TR_STEP
    const float s = rbf(rbf(x[0]) * SCALE);
    float pm = s;
#pragma unroll
    for (int mm = 32; mm >= 1; mm >>= 1) pm = fmaxf(pm, __shfl_xor(pm, mm));
    const float mn = fmaxf(m, pm);
    const float alpha = __builtin_amdgcn_exp2f((m - mn) * LOG2E);
    const float p = __builtin_amdgcn_exp2f((s - mn) * LOG2E);
    l = fmaf(l, alpha, p); m = mn;
#pragma unroll
    for (int k = 0; k < 8; ++k) acc[k] *= alpha;
#pragma unroll
    for (int t = 0; t < 16; ++t) {
      const float pt = __shfl(p, (lane & 48) | t);
#pragma unroll
      for (int k = 0; k < 4; ++k) { acc[2 * k] = fmaf(pt, lo16(vv[t][k]), acc[2 * k]); acc[2 * k + 1] = fmaf(pt, hi16(vv[t][k]), acc[2 * k + 1]); }
    }
  }
#pragma unroll
  for (int k = 0; k < 8; ++k) { acc[k] += __shfl_xor(acc[k], 16); acc[k] += __shfl_xor(acc[k], 32); }
  l = wave_sum(l);
  if (r == 0) {
#pragma unroll
    for (int k = 0; k < 8; ++k) mrg[wave][4 + 8 * c + k] = acc[k];
    if (c == 0) { mrg[wave][0] = m; mrg[wave][1] = l; } }
  __syncthreads();
  if (tid < 128) {
    float M = -INFINITY;
#pragma unroll
    for (int w = 0; w < AW; ++w) M = fmaxf(M, mrg[w][0]);
    float L = 0.f, o = 0.f;
#pragma unroll
    for (int w = 0; w < AW; ++w) { const float wt = __builtin_amdgcn_exp2f((mrg[w][0] - M) * LOG2E); L = fmaf(wt, mrg[w][1], L); o = fmaf(wt, mrg[w][4 + tid], o); }
    float* pp = part + (size_t)blockIdx.x * 132;
    pp[4 + tid] = o;
    if (tid == 0) { pp[0] = M; pp[1] = L; }
  }
}

__global__ void __launch_bounds__(256) k_wo(const float* __restrict__ part, const u16* __restrict__ wo, float* __restrict__ wop) {
  __shared__ float os[DH]; __shared__ float red[32 * 64]; __shared__ float out[64];
  const int head = blockIdx.x / 8, cg = blockIdx.x % 8, tid = threadIdx.x;
  constexpr float LOG2E = 1.4426950408889634f;
  if (tid < 128) {
    const float* pp = part + (size_t)head * NSPLIT * 132;
    float M = -INFINITY;
#pragma unroll
    for (int s = 0; s < NSPLIT; ++s) M = fmaxf(M, pp[s * 132]);
    float L = 0.f, o = 0.f;
#pragma unroll
    for (int s = 0; s < NSPLIT; ++s) { const float wt = __builtin_amdgcn_exp2f((pp[s * 132] - M) * LOG2E); L = fmaf(wt, pp[s * 132 + 1], L); o = fmaf(wt, pp[s * 132 + 4 + tid], o); }
    os[tid] = rbf(o / L);
  }
  __syncthreads();
  gemv64<DH>(wo, D, head * DH, cg * 64, os, red, out);
  if (tid < 64) wop[head * D + cg * 64 + tid] = out[tid];
}

__device__ __forceinline__ float gelu_tanh(float g) {
  const float u = 0.7978845608028654f * (g + 0.044715f * g * g * g);
  return 0.5f * g * (1.f + tanhf(u));
}

__global__ void __launch_bounds__(256) k_gu(const u16* __restrict__ x, const u16* __restrict__ mod, const float* __restrict__ wop,
                                            const u16* __restrict__ wg, const u16* __restrict__ wu, u16* __restrict__ x1, u16* __restrict__ h2) {
  __shared__ float xs[D]; __shared__ float hs[D]; __shared__ float red[32 * 64]; __shared__ float og[64]; __shared__ float ou[64];
  for (int i = threadIdx.x; i < D; i += 256) {
    float s = 0.f;
#pragma unroll
    for (int h = 0; h < H; ++h) s += wop[h * D + i];
    const float v = rbf(bf2f(x[i]) + rbf(bf2f(mod[2 * D + i]) * rbf(s)));
    xs[i] = v;
    if (blockIdx.x == 0) x1[i] = f2bf(v);
  }
  __syncthreads();
  adaln(xs, mod + 3 * D, mod + 4 * D, hs, red);
  gemv64<D>(wg, DF, 0, blockIdx.x * 64, hs, red, og);
  gemv64<D>(wu, DF, 0, blockIdx.x * 64, hs, red, ou);
  if (threadIdx.x < 64) h2[blockIdx.x * 64 + threadIdx.x] = f2bf(rbf(gelu_tanh(rbf(og[threadIdx.x]))) * rbf(ou[threadIdx.x]));
}

__global__ void __launch_bounds__(256) k_down(const u16* __restrict__ x1, const u16* __restrict__ mod, const u16* __restrict__ h2,
                                              const u16* __restrict__ wd, u16* __restrict__ outp) {
  __shared__ float hs[DF]; __shared__ float red[32 * 64]; __shared__ float out[64];
  for (int i = threadIdx.x; i < DF; i += 256) hs[i] = bf2f(h2[i]);
  __syncthreads();
  gemv64<DF>(wd, D, 0, blockIdx.x * 64, hs, red, out);
  if (threadIdx.x < 64) { const int e = blockIdx.x * 64 + threadIdx.x;
    outp[e] = f2bf(bf2f(x1[e]) + rbf(bf2f(mod[5 * D + e]) * rbf(out[threadIdx.x]))); }
}

extern "C" void kernel_launch(void* const* d_in, const int* in_sizes, int n_in, void* d_out, int out_size, void* d_ws, size_t ws_size, hipStream_t stream) {
  const u16* x = (const u16*)d_in[0]; const u16* K = (const u16*)d_in[1]; const u16* V = (const u16*)d_in[2];
  const u16* wq = (const u16*)d_in[3]; const u16* wo = (const u16*)d_in[4];
  const u16* wg = (const u16*)d_in[6]; const u16* wu = (const u16*)d_in[7]; const u16* wd = (const u16*)d_in[8];
  const u16* c = (const u16*)d_in[9]; const u16* wmod = (const u16*)d_in[10];
  char* ws = (char*)d_ws;
  u16* mod = (u16*)(ws); u16* q = (u16*)(ws + 8192); u16* x1 = (u16*)(ws + 8192 + 8192); u16* h2 = (u16*)(ws + 8192 * 3);
  float* part = (float*)(ws + 65536); float* wop = (float*)(ws + 65536 + 262144);
  k_mod<<<48, 256, 0, stream>>>(c, wmod, mod);
  k_q<<<64, 256, 0, stream>>>(x, mod, wq, q);
  k_attn<<<H * NSPLIT, AW * 64, 0, stream>>>(q, K, V, part);
  k_wo<<<256, 256, 0, stream>>>(part, wo, wop);
  k_gu<<<28, 256, 0, stream>>>(x, mod, wop, wg, wu, x1, h2);
  k_down<<<8, 256, 0, stream>>>(x1, mod, h2, wd, (u16*)d_out);
}
```

```cpp
#include <hip/hip_runtime.h>
#include <stdint.h>

typedef unsigned short u16;
typedef unsigned long long u64;
typedef unsigned u32x4 __attribute__((ext_vector_type(4)));
typedef float f32x2 __attribute__((ext_vector_type(2)));
typedef double f64x2 __attribute__((ext_vector_type(2)));
typedef __bf16 bf16x2 __attribute__((ext_vector_type(2)));
#define GAS __attribute__((address_space(1)))
#define LAS __attribute__((address_space(3)))
#define RLX_AGENT __ATOMIC_RELAXED, __HIP_MEMORY_SCOPE_AGENT

constexpr int NSK = 120;
constexpr int ND = 3;
constexpr int LOOK = 4;
constexpr int TAU = 5420;
constexpr int BURST = 3;
constexpr int TOFF = 190;
constexpr int TBIG = 8;
constexpr int N4 = 8;
constexpr int N2 = 12;
constexpr int N1 = 8;
constexpr int CHKAT = 48, CODEAT = 60;
constexpr unsigned CODE_LEN = 22528;

constexpr int D = 512, H = 32, SK = 65536, DH = 128, DF = 1792;
constexpr int NWG = 256, NTHR = 384;
constexpr int NRK = NSK - 4 * ND, NRS = NRK + NSK;
constexpr int PP = 2 * (128 - NSK);
constexpr int PF = 8 * PP;
constexpr int NBIG = (PF - 4 * N4 - 2 * N2 - N1) / TBIG;
static_assert(NSK % 4 == 0 && PP % TBIG == 0 && NBIG >= 0 && NBIG * TBIG + 4 * N4 + 2 * N2 + N1 == PF, "ticket geometry");
constexpr float SCALE = 0.08838834764831845f, LOG2E = 1.4426950408889634f;
constexpr unsigned SENT = 0xFFFFFFFFu;

constexpr size_t W_EPOCH = 0, W_FLAGB = 0x2000, W_CNT = 0x4000, W_MODV = 0x8000, W_ML = 0xA000, W_H2 = 0xC000, W_X1 = 0xD000,
                 W_R0 = 0x10000, W_WOP = 0x40000, W_R2 = 0x60000, W_R1 = 0x80000, W_USED = 0xC0000;
constexpr int L_SC = 131072, L_RED1 = L_SC + NSK * 128, L_RED2 = L_RED1 + 2048, L_MRG = L_RED2 + 2048, L_QS = L_MRG + 4 * 1040,
              L_CTL = L_QS + 256, L_END = L_CTL + 256;
static_assert(L_END <= 160 * 1024 && L_RED1 % 16 == 0, "LDS budget");
constexpr int C_LANDED = 0, C_TOTAL = 1, C_DONE = 4, C_ISSUED = 8, C_POSTED = 9, C_QRDY = 10, C_POSTOK = 13,
              C_TK = 16, C_T = 32;
constexpr u64 TMO = 4000000;

__device__ __forceinline__ float bf2f(unsigned v) { return __uint_as_float(v << 16); }
__device__ __forceinline__ float lo16(unsigned w) { return __uint_as_float(w << 16); }
__device__ __forceinline__ float hi16(unsigned w) { return __uint_as_float(w & 0xffff0000u); }
__device__ __forceinline__ float rbf(float f) { return (float)(__bf16)f; }
__device__ __forceinline__ unsigned pk2(float a, float b) { bf16x2 t = {(__bf16)a, (__bf16)b}; return __builtin_bit_cast(unsigned, t); }
__device__ __forceinline__ float dot2(unsigned a, unsigned b, float acc) {
  return __builtin_amdgcn_fdot2_f32_bf16(__builtin_bit_cast(bf16x2, a), __builtin_bit_cast(bf16x2, b), acc, false);
}
template <int CTRL> __device__ __forceinline__ unsigned dppu(unsigned x) { return (unsigned)__builtin_amdgcn_mov_dpp((int)x, CTRL, 0xf, 0xf, true); }
template <int CTRL> __device__ __forceinline__ float dpp(float x) { return __uint_as_float(dppu<CTRL>(__float_as_uint(x))); }
constexpr int XOR1 = 0xB1, XOR2 = 0x4E, XOR7 = 0x141, ROR8 = 0x128, ROR4 = 0x124, BC0 = 0x150;
__device__ __forceinline__ float sw32(float a, float b) {
  auto s = __builtin_amdgcn_permlane32_swap(__float_as_uint(a), __float_as_uint(b), false, false);
  return __uint_as_float(s[0]) + __uint_as_float(s[1]);
}
__device__ __forceinline__ float sw16(float a, float b) {
  auto s = __builtin_amdgcn_permlane16_swap(__float_as_uint(a), __float_as_uint(b), false, false);
  return __uint_as_float(s[0]) + __uint_as_float(s[1]);
}
__device__ __forceinline__ f32x2 red8to2(const float (&v)[8]) {
  const float n0 = sw32(v[0], v[4]), n1 = sw32(v[1], v[5]), n2 = sw32(v[2], v[6]), n3 = sw32(v[3], v[7]);
  f32x2 r = {sw16(n0, n2), sw16(n1, n3)};
  return r;
}
__device__ __forceinline__ float row_sum(float v) { v += dpp<ROR8>(v); v += dpp<ROR4>(v); v += dpp<XOR2>(v); v += dpp<XOR1>(v); return v; }
__device__ __forceinline__ float wave_sum(float v) { v = row_sum(v); v = sw16(v, v); return sw32(v, v); }
__device__ __forceinline__ float wave_max(float v) {
  v = fmaxf(v, dpp<ROR8>(v)); v = fmaxf(v, dpp<ROR4>(v)); v = fmaxf(v, dpp<XOR2>(v)); v = fmaxf(v, dpp<XOR1>(v));
  auto s = __builtin_amdgcn_permlane16_swap(__float_as_uint(v), __float_as_uint(v), false, false);
  v = fmaxf(__uint_as_float(s[0]), __uint_as_float(s[1]));
  auto t = __builtin_amdgcn_permlane32_swap(__float_as_uint(v), __float_as_uint(v), false, false);
  return fmaxf(__uint_as_float(t[0]), __uint_as_float(t[1]));
}
__device__ __forceinline__ double mkd(unsigned lo, unsigned hi) { return __builtin_bit_cast(double, (u64)lo | ((u64)hi << 32)); }
__device__ __forceinline__ unsigned dlo(double d) { return (unsigned)__builtin_bit_cast(u64, d); }
__device__ __forceinline__ unsigned dhi(double d) { return (unsigned)(__builtin_bit_cast(u64, d) >> 32); }
template <int CTRL> __device__ __forceinline__ double dppd(double x) { return mkd(dppu<CTRL>(dlo(x)), dppu<CTRL>(dhi(x))); }
__device__ __forceinline__ double sw32d(double a, double b) {
  auto s = __builtin_amdgcn_permlane32_swap(dlo(a), dlo(b), false, false);
  auto t = __builtin_amdgcn_permlane32_swap(dhi(a), dhi(b), false, false);
  return mkd(s[0], t[0]) + mkd(s[1], t[1]);
}
__device__ __forceinline__ double sw16d(double a, double b) {
  auto s = __builtin_amdgcn_permlane16_swap(dlo(a), dlo(b), false, false);
  auto t = __builtin_amdgcn_permlane16_swap(dhi(a), dhi(b), false, false);
  return mkd(s[0], t[0]) + mkd(s[1], t[1]);
}
__device__ __forceinline__ unsigned plo(unsigned rowA, unsigned rowB) { return __builtin_amdgcn_perm(rowB, rowA, 0x05040100u); }
__device__ __forceinline__ unsigned phi(unsigned rowA, unsigned rowB) { return __builtin_amdgcn_perm(rowB, rowA, 0x07060302u); }
__device__ __forceinline__ unsigned canon(float f) { return min(__float_as_uint(f), 0xFFFFFFFEu); }
__device__ __forceinline__ unsigned canonu(unsigned u) { return min(u, 0xFFFFFFFEu); }
__device__ __forceinline__ u64 mk64(unsigned lo, unsigned hi) { return (u64)lo | ((u64)hi << 32); }
__device__ __forceinline__ u64 rt() { return __builtin_amdgcn_s_memrealtime(); }
__device__ __forceinline__ u64 gld64(const unsigned char* p) { return __hip_atomic_load((const GAS u64*)p, RLX_AGENT); }
__device__ __forceinline__ unsigned gld32(const unsigned char* p) { return __hip_atomic_load((const GAS unsigned*)p, RLX_AGENT); }
__device__ __forceinline__ void gst64(unsigned char* p, u64 v) { __hip_atomic_store((GAS u64*)p, v, RLX_AGENT); }
__device__ __forceinline__ void gst32(unsigned char* p, unsigned v) { __hip_atomic_store((GAS unsigned*)p, v, RLX_AGENT); }
__device__ __forceinline__ int uni(unsigned v) { return __builtin_amdgcn_readfirstlane(v); }
#define DRAIN() asm volatile("s_waitcnt vmcnt(0)" ::: "memory")
#define LDSW() asm volatile("s_waitcnt lgkmcnt(0)" ::: "memory")
#define CFENCE() asm volatile("" ::: "memory")
#define SCHED() __builtin_amdgcn_sched_barrier(0)
struct Spin { u64 t0; unsigned n;
  __device__ __forceinline__ Spin() : t0(0), n(0) {}
  __device__ __forceinline__ bool out() { if ((++n & 1023u) != 0u) return false; const u64 t = rt(); if (t0 == 0) { t0 = t; return false; } return t - t0 > TMO; } };

__device__ __forceinline__ float ln_den(float var) { return __builtin_amdgcn_rcpf(rbf(__builtin_amdgcn_sqrtf(rbf(rbf(var) + 1e-5f)))); }
__device__ __forceinline__ float modulate(float x, float mb, float den, float scale, float shift) {
  const float n = rbf(rbf(x - mb) * den);
  return rbf(rbf(n * rbf(1.f + scale)) + shift);
}
__device__ __forceinline__ float silu_bf(float v) { return rbf(v / (1.f + expf(-v))); }


__device__ __forceinline__ void dma4(unsigned voff, const unsigned char* src, unsigned ldst) {
  unsigned keep;
  asm volatile("s_mov_b32 %0, m0\n\ts_mov_b32 m0, %3\n\ts_nop 0\n\t"
               "global_load_lds_dwordx4 %1, %2 nt\n\t"
               "global_load_lds_dwordx4 %1, %2 offset:1024 nt\n\t"
               "global_load_lds_dwordx4 %1, %2 offset:2048 nt\n\t"
               "global_load_lds_dwordx4 %1, %2 offset:3072 nt\n\t"
               "s_mov_b32 m0, %0"
               : "=&s"(keep) : "v"(voff), "s"(src), "s"(ldst) : "memory");
}

#define RB8(w, o) asm volatile("" : "+v"(w[o]), "+v"(w[o + 1]), "+v"(w[o + 2]), "+v"(w[o + 3]), "+v"(w[o + 4]), "+v"(w[o + 5]), "+v"(w[o + 6]), "+v"(w[o + 7]) : : "memory")

template <int T> __device__ __forceinline__ void pv_row(float (&acc)[8], float p, const u32x4& v) {
  const float pt = dpp<BC0 + T>(p);
#pragma unroll
  for (int k = 0; k < 4; ++k) { acc[2 * k] = fmaf(pt, lo16(v[k]), acc[2 * k]); acc[2 * k + 1] = fmaf(pt, hi16(v[k]), acc[2 * k + 1]); }
}
template <int T> __device__ __forceinline__ void pv_row64(double (&acc)[8], double p, const u32x4& v) {
  const double pt = dppd<BC0 + T>(p);
#pragma unroll
  for (int k = 0; k < 4; ++k) { acc[2 * k] = fma(pt, (double)lo16(v[k]), acc[2 * k]); acc[2 * k + 1] = fma(pt, (double)hi16(v[k]), acc[2 * k + 1]); }
}
template <int T, int N, int STEP, int OFF> struct PV { static __device__ __forceinline__ void run(float (&acc)[8], float p, const u32x4 (&w)[16]) {
  pv_row<T * STEP>(acc, p, w[OFF + T]); PV<T + 1, N, STEP, OFF>::run(acc, p, w); } };
template <int N, int STEP, int OFF> struct PV<N, N, STEP, OFF> { static __device__ __forceinline__ void run(float (&)[8], float, const u32x4 (&)[16]) {} };

template <int I, int N, int MODE, int NW> struct GV { static __device__ __forceinline__ void run(float (&a)[8], const unsigned* in, const u32x4 (&w)[NW]) {
  unsigned s;
  if constexpr (MODE == 0) s = dppu<BC0 + I>(in[0]);
  else if constexpr (MODE == 1) { s = (unsigned)__builtin_amdgcn_update_dpp(0, (int)in[0], BC0 + I, 0xf, 0x3, false); s = (unsigned)__builtin_amdgcn_update_dpp((int)s, (int)in[0], BC0 + 8 + I, 0xf, 0xc, false); }
  else s = dppu<(I & 3) * 0x55>(in[I >> 2]);
#pragma unroll
  for (int k = 0; k < 8; ++k) a[k] = dot2(w[2 * I + (k >> 2)][k & 3], s, a[k]);
  GV<I + 1, N, MODE, NW>::run(a, in, w); } };
template <int N, int MODE, int NW> struct GV<N, N, MODE, NW> { static __device__ __forceinline__ void run(float (&)[8], const unsigned*, const u32x4 (&)[NW]) {} };
template <int NR> __device__ __forceinline__ void pair_rows(u32x4 (&r)[NR]) {
#pragma unroll
  for (int i = 0; i < NR / 2; ++i) { const u32x4 ra = r[2 * i], rb = r[2 * i + 1];
    r[2 * i] = u32x4{plo(ra[0], rb[0]), phi(ra[0], rb[0]), plo(ra[1], rb[1]), phi(ra[1], rb[1])};
    r[2 * i + 1] = u32x4{plo(ra[2], rb[2]), phi(ra[2], rb[2]), plo(ra[3], rb[3]), phi(ra[3], rb[3])};
    asm volatile("" : "+v"(r[2 * i]), "+v"(r[2 * i + 1])); }
}
__device__ __forceinline__ void put_granule(__amdgpu_buffer_rsrc_t rs, int off, f32x2 v, u64 T) {
  const u32x4 g = {__float_as_uint(v[0]), __float_as_uint(v[0]) ^ (unsigned)T, __float_as_uint(v[1]), __float_as_uint(v[1]) ^ (unsigned)(T >> 32)};
  __builtin_amdgcn_raw_buffer_store_b128(g, rs, off, 0, 16);
}
__device__ __forceinline__ f32x2 sum8_granules(__amdgpu_buffer_rsrc_t rs, int off, u64 T) {
  u32x4 g[8]; Spin sp;
  for (;;) {
    CFENCE();
#pragma unroll
    for (int k = 0; k < 8; ++k) g[k] = __builtin_amdgcn_raw_buffer_load_b128(rs, off + (k & 3) * 1024, (k >> 2) * 4096, 16);
    SCHED();
    unsigned bad = 0;
#pragma unroll
    for (int k = 0; k < 8; ++k) bad |= (g[k][0] ^ g[k][1] ^ (unsigned)T) | (g[k][2] ^ g[k][3] ^ (unsigned)(T >> 32));
    if (__all(bad == 0u) || sp.out()) break;
    __builtin_amdgcn_s_sleep(4);
  }
  f32x2 p[8];
#pragma unroll
  for (int k = 0; k < 8; ++k) { p[k][0] = __uint_as_float(g[k][0]); p[k][1] = __uint_as_float(g[k][2]); }
  return ((p[0] + p[1]) + (p[2] + p[3])) + ((p[4] + p[5]) + (p[6] + p[7]));
}

extern "C" __global__ void __launch_bounds__(NTHR, 2)
fused_decode(const u16* __restrict__ x, const u16* __restrict__ K, const u16* __restrict__ V, const u16* __restrict__ wq, const u16* __restrict__ wo,
             const u16* __restrict__ wg, const u16* __restrict__ wu, const u16* __restrict__ wd, const u16* __restrict__ cvec,
             const u16* __restrict__ wmod, u16* __restrict__ out, unsigned char* __restrict__ ws) {
  extern __shared__ __attribute__((aligned(16))) unsigned char lds_[];
  LAS unsigned char* L = (LAS unsigned char*)lds_;
  volatile LAS unsigned* C = (volatile LAS unsigned*)(L + L_CTL);
  const int tid = threadIdx.x, lane = tid & 63, wave = uni(tid >> 6), b = blockIdx.x, head = b >> 3, j = b & 7;
  const u64 tstart = rt();
  if (wave == 0) { C[lane] = 0u; LDSW(); }
  __builtin_amdgcn_s_barrier();
  SCHED();

  if (wave == 0) {
    __builtin_amdgcn_s_setprio(3);
    const unsigned char* Kb = (const unsigned char*)K + (size_t)head * (SK * DH * 2);
    const unsigned char* Vb = (const unsigned char*)V + (size_t)head * (SK * DH * 2);
    const unsigned voff = lane * 16, lbase = (unsigned)(uintptr_t)L;
    const size_t st0 = (size_t)j * (128 * 16384);
    auto issue = [&](const unsigned char* A, const unsigned char* B, int f) {
      const unsigned dst = lbase + (f & 7) * 16384;
      dma4(voff, A, dst); dma4(voff, A + 4096, dst + 4096); dma4(voff, B, dst + 8192); dma4(voff, B + 4096, dst + 12288); };
    int f = 0, pub = 0; unsigned taken = 0, key = 0, rem = 0; bool dead = false;
    u64 nexti = tstart * 100 + (TOFF * 100 + 4 * ND * TAU);
#pragma unroll 1
    for (;;) {
      const unsigned char *A, *B;
      if (f < NRS) { A = (f < NRK ? Kb + (size_t)(f + 4 * ND) * 16384 : Vb + (size_t)(f - NRK) * 16384) + st0; B = A + 8192; }
      else {
        if (rem == 0) {
          if ((unsigned)uni(C[C_POSTED]) <= taken) {
            DRAIN(); pub = f; C[C_LANDED] = f;
            Spin sp; while ((unsigned)uni(C[C_POSTED]) <= taken) { if (sp.out()) { dead = true; break; } }
            if (dead) break;
          }
          const unsigned t = uni(C[C_TK + (taken & 15)]); ++taken;
          key = t & 0xffffu; rem = t >> 16;
          if (rem == 0) break;
        }
        A = Kb + (size_t)key * 256; B = Vb + (size_t)key * 256; key += 32; --rem;
      }
      auto released = [&]() -> bool {
        if (f - 8 < NRS) return uni(C[C_DONE + (f & 3)]) >= f - 7;
        const u32x4 d4 = *(volatile LAS u32x4*)(L + L_CTL + C_DONE * 4);
        return uni(min(min(d4[0], d4[1]), min(d4[2], d4[3]))) >= f - 7; };
      if (f >= 8 && !released()) {
        DRAIN(); pub = f; C[C_LANDED] = f;
        Spin sp; while (!released()) { if (sp.out()) { dead = true; break; } }
        if (dead) break;
      }
      if (f < NRS) {
        u64 now;
        while ((now = rt() * 100) < nexti) {}
        nexti = max(nexti, now - BURST * TAU / 10) + TAU;
      }
      issue(A, B, f);
      ++f; C[C_ISSUED] = f;
      asm volatile("s_waitcnt vmcnt(16)" ::: "memory");
      if (f - 1 > pub) { pub = f - 1; C[C_LANDED] = pub; }
    }
    C[C_TOTAL] = f;
    DRAIN();
    C[C_LANDED] = f;
    return;
  }

  const int tr = (head + 4 * j) & 31;
  const int hg = head >> 2, dd = (j - 2 * (head & 3) - hg) & 7;
  const int ub = (dd == 4 ? 16 : 0) + (head >> 1), n6 = 2 * j + ((head >> 1) & 1);
  const int role = dd < 2 ? 3 : dd == 2 ? ((head & 1) ? 2 : 1) : (dd == 4 && !(head & 1) && ub < 28) ? 1 : 0;

  if (wave == 1) {
    const int r = lane >> 4, c = lane & 15;
    const bool first = tr >= 14 && tr < 22, prod = tr < 22 || tr >= 30;
    const int t2 = j * 16 + (tr < 14 ? tr : tr - 16), rr = first ? tr - 14 : t2 & 7;
    unsigned vz; asm volatile("v_mov_b32 %0, 0" : "=v"(vz));
    const u64 Ev = gld64(ws + W_EPOCH + vz);
    const u32x4 xv = *(const u32x4*)(x + 8 * lane);
    const unsigned xe = *(const unsigned*)(x + 64 * j + 16 * r + 2 * (c & 7));
    const unsigned cp = *(const unsigned*)(cvec + 64 * rr + 16 * r + 2 * (c & 7));
    u32x4 r0[16], r1[16];
    if (prod) {
      const __amdgpu_buffer_rsrc_t rm = __builtin_amdgcn_make_buffer_rsrc((void*)wmod, 0, D * 6 * D * 2, 0x00020000);
      const int vo = ((64 * rr + 16 * r) * (6 * D) + (first ? (c >> 3) * D + 64 * j + 8 * (c & 7) : 2 * D + 128 * (t2 >> 3) + 8 * c)) * 2;
#pragma unroll
      for (int i = 0; i < 16; ++i) r0[i] = __builtin_amdgcn_raw_buffer_load_b128(rm, vo, i * (6 * D * 2), 2);
    }
    {
      const __amdgpu_buffer_rsrc_t rq = __builtin_amdgcn_make_buffer_rsrc((void*)wq, 0, D * H * DH * 2, 0x00020000);
      const int vq = ((64 * j + 16 * r) * (H * DH) + head * DH + 8 * c) * 2;
#pragma unroll
      for (int i = 0; i < 16; ++i) r1[i] = __builtin_amdgcn_raw_buffer_load_b128(rq, vq, i * (H * DH * 2), 2);
    }
    SCHED();
    const u64 E = mk64(uni((unsigned)Ev), uni((unsigned)(Ev >> 32)));
    u64 z = E + 0x9E3779B97F4A7C15ull; z = (z ^ (z >> 30)) * 0xBF58476D1CE4E5B9ull; z = (z ^ (z >> 27)) * 0x94D049BB133111EBull;
    const u64 T = z ^ (z >> 31);
    const __amdgpu_buffer_rsrc_t rw = __builtin_amdgcn_make_buffer_rsrc((void*)ws, 0, (int)W_USED, 0x00020000);
    const int pidx = 2 * (lane >> 5) + ((lane >> 4) & 1);
    float a[8];
    if (prod) {
      const unsigned sp_ = pk2(silu_bf(lo16(cp)), silu_bf(hi16(cp)));
      pair_rows<16>(r0);
#pragma unroll
      for (int k = 0; k < 8; ++k) a[k] = 0.f;
      GV<0, 8, 0, 16>::run(a, &sp_, r0);
      const int p = 4 * (c & 7) + pidx;
      put_granule(rw, first ? (int)W_R0 + (j * 8 + rr) * 1024 + (16 * (p >> 3) + (c & 8) + (p & 7)) * 16 : (int)W_R2 + t2 * 1024 + (4 * c + pidx) * 16, red8to2(a), T);
    }
    float s = 0.f;
#pragma unroll
    for (int k = 0; k < 4; ++k) s += lo16(xv[k]) + hi16(xv[k]);
    const float mean = wave_sum(s) * (1.f / D);
    float q2 = 0.f;
#pragma unroll
    for (int k = 0; k < 4; ++k) { const float a0 = lo16(xv[k]) - mean, a1 = hi16(xv[k]) - mean; q2 = fmaf(a0, a0, q2); q2 = fmaf(a1, a1, q2); }
    const float den = rbf(sqrtf(rbf(rbf(wave_sum(q2) * (1.f / D)) + 1e-5f))), mb = rbf(mean);
    float n0 = rbf(rbf(lo16(xe) - mb) / den), n1 = rbf(rbf(hi16(xe) - mb) / den);
    pair_rows<16>(r1);
    asm volatile("" : "+v"(n0), "+v"(n1));
    if (lane == 0) *(volatile LAS u64*)(L + L_CTL + C_T * 4) = T;
    SCHED();
    const f32x2 sc = sum8_granules(rw, (int)W_R0 + j * 8192 + lane * 16, T);
    const float u0 = rbf(1.f + rbf(dpp<ROR8>(sc[0]))), u1 = rbf(1.f + rbf(dpp<ROR8>(sc[1])));
    const unsigned hp = pk2(rbf(n0 * u0) + rbf(sc[0]), rbf(n1 * u1) + rbf(sc[1]));
#pragma unroll
    for (int k = 0; k < 8; ++k) a[k] = 0.f;
    GV<0, 8, 0, 16>::run(a, &hp, r1);
    put_granule(rw, (int)W_R1 + b * 1024 + (4 * c + pidx) * 16, red8to2(a), T);
    const u64 S2 = ~0ull;
    if (lane < 2) gst64(ws + W_ML + b * 16 + lane * 8, S2);
    if (role == 3 && lane < 32) gst64(ws + W_WOP + hg * 2048 + j * 256 + lane * 8, S2);
    if (role == 1 && lane < 16) gst64(ws + W_H2 + ub * 128 + lane * 8, S2);
    if (role == 1 && ub == 0) { gst64(ws + W_X1 + lane * 16, S2); gst64(ws + W_X1 + lane * 16 + 8, S2); }
    if (j == 0 && lane == 0) gst32(ws + W_CNT + head * 256, 0u);
    const f32x2 q = sum8_granules(rw, (int)W_R1 + head * 8192 + lane * 16, T);
    *(LAS unsigned*)(L + L_QS + lane * 4) = pk2(q[0], q[1]);
    LDSW();
    C[C_QRDY] = 1u;
    if (prod && !first && rr == 0) {
      const f32x2 mv = sum8_granules(rw, (int)W_R2 + (t2 >> 3) * 8192 + lane * 16, T);
      gst64(ws + W_MODV + (128 * (t2 >> 3) + 2 * lane) * 4, mk64(__float_as_uint(rbf(mv[0])), __float_as_uint(rbf(mv[1]))));
    }
    DRAIN();
    if (lane == 0) gst64(ws + W_FLAGB + b * 8, T);
    { Spin sp; while (uni(C[C_ISSUED]) < CHKAT) { __builtin_amdgcn_s_sleep(16); if (sp.out()) break; } }
    { Spin sp;
      for (;;) {
        bool ok = true;
#pragma unroll
        for (int k = 0; k < 4; ++k) ok &= gld64(ws + W_FLAGB + (k * 64 + lane) * 8) == T;
        if (__all(ok) || sp.out()) break;
        __builtin_amdgcn_s_sleep(64);
      } }
    C[C_POSTOK] = 1u;
    {
      { Spin sp; while (uni(C[C_ISSUED]) < NRS - CODEAT) { __builtin_amdgcn_s_sleep(16); if (sp.out()) break; } }
      u64 pc; unsigned rel;
      asm volatile("s_getpc_b64 %0\n.Lfd_here:\n\ts_mov_b32 %1, .Lfd_here-fused_decode" : "=&s"(pc), "=&s"(rel));
      const u32x4 c0 = *(const GAS u32x4*)(pc - rel + min(head * 1024u + lane * 16u, CODE_LEN - 16u));
      asm volatile("" :: "v"(c0));
    }
    unsigned posted = 0; int known = NRS;
    for (;;) {
      { Spin sp; while (known - uni(C[C_ISSUED]) >= LOOK) { __builtin_amdgcn_s_sleep(2); if (sp.out()) break; } }
      unsigned t = 0;
      if (lane == 0) t = __hip_atomic_fetch_add((GAS unsigned*)(ws + W_CNT + head * 256), 1u, RLX_AGENT);
      t = uni(t);
      unsigned st, len;
      if (t < NBIG) { st = t * TBIG; len = TBIG; }
      else if (t < NBIG + N4) { st = NBIG * TBIG + (t - NBIG) * 4; len = 4; }
      else if (t < NBIG + N4 + N2) { st = NBIG * TBIG + 4 * N4 + (t - NBIG - N4) * 2; len = 2; }
      else if (t < NBIG + N4 + N2 + N1) { st = NBIG * TBIG + 4 * N4 + 2 * N2 + (t - NBIG - N4 - N2); len = 1; }
      else { st = 0; len = 0; }
      C[C_TK + (posted & 15)] = ((st / PP) * 8192 + NSK * 64 + (st % PP) * 32) | (len << 16); LDSW(); ++posted; C[C_POSTED] = posted; known += len;
      if (len == 0) break;
    }
    return;
  }

  const int cw = wave - 2, r = lane >> 4, c = lane & 15;
  const int sub2 = (lane >> 5) * 4 + ((lane >> 4) & 1) * 2;
  const int e2 = 128 * cw + 2 * lane;
  unsigned xp = *(const unsigned*)(x + e2);
  u32x4 wd_[ND][16];
  {
    const u32x4* kd = (const u32x4*)((const unsigned char*)K + (size_t)head * (SK * DH * 2) + (size_t)j * (128 * 16384) + cw * 16384) + lane;
#pragma unroll
    for (int i = 0; i < ND; ++i) {
      { const u64 tg = tstart + (4 * i + cw < 2 ? 0 : TOFF + ((4 * i + cw) * TAU) / 100); while (rt() < tg) {} }
#pragma unroll
      for (int t = 0; t < 16; ++t) wd_[i][t] = __builtin_nontemporal_load(kd + i * 4096 + t * 64);
      SCHED();
    }
  }
  asm volatile("" : "+v"(xp));
  { Spin sp; while (uni(C[C_QRDY]) == 0) { __builtin_amdgcn_s_sleep(1); if (sp.out()) break; } }
  CFENCE();
  const u32x4 qv = *(const LAS u32x4*)(L + L_QS + c * 16);

  int total_seen = 0;
  auto wait_fill = [&](int f) -> bool {
    Spin sp;
    for (;;) {
      const u64 v = *(volatile LAS u64*)(L + L_CTL);
      const int landed = uni((unsigned)v), total = uni((unsigned)(v >> 32));
      if (landed > f) { total_seen = total; CFENCE(); return true; }
      if ((total != 0 && f >= total) || sp.out()) return false;
      if (f < NRS) __builtin_amdgcn_s_sleep(10);
    } };
  auto read_slot = [&](int f, u32x4 (&w)[16]) {
    const LAS u32x4* src = (const LAS u32x4*)(L + (f & 7) * 16384) + lane;
#pragma unroll
    for (int t = 0; t < 16; ++t) w[t] = src[t * 64];
    LDSW();
    C[C_DONE + cw] = f + 1;
    RB8(w, 0); RB8(w, 8); };
#define TR_STEP(HALF, CTRL, BIT) _Pragma("unroll") for (int h_ = 0; h_ < (HALF); ++h_) { \
    const float lo_ = xs[h_], hi_ = xs[h_ + (HALF)]; const float t1_ = lo_ + dpp<CTRL>(lo_), t2_ = hi_ + dpp<CTRL>(hi_); \
    xs[h_] = (c & (BIT)) ? t2_ : t1_; }

  LAS u16* SC = (LAS u16*)(L + L_SC) + lane;
  float m = -INFINITY;
  auto score64 = [&](const u32x4 (&w)[16], int k) {
    float xs[16];
#pragma unroll
    for (int t = 0; t < 16; ++t) xs[t] = dot2(w[t][3], qv[3], dot2(w[t][2], qv[2], dot2(w[t][1], qv[1], dot2(w[t][0], qv[0], 0.f))));
    TR_STEP(8, ROR8, 8) TR_STEP(4, XOR7, 4) TR_STEP(2, XOR2, 2) TR_STEP(1, XOR1, 1)
    const float s = rbf(rbf(xs[0]) * SCALE);
    m = fmaxf(m, s);
    SC[k * 64] = (u16)(__float_as_uint(s) >> 16); };
#pragma unroll
  for (int i = 0; i < ND; ++i) score64(wd_[i], 4 * i + cw);
#pragma unroll 1
  for (int f = cw; f < NRK; f += 4) {
    if (!wait_fill(f)) break;
    u32x4 w[16]; read_slot(f, w);
    score64(w, f + 4 * ND);
  }
  float nref = ceilf(fmaxf(wave_max(m) * LOG2E, -1.0e6f));
  const int g = lane >> 2, q4 = lane & 3, hw = (head & ~3) + cw;
  { Spin sp; while (uni(C[C_POSTOK]) == 0) { if (sp.out()) break; } }
  CFENCE();
  const u16* const wp_ = role == 1 ? (c < 8 ? wg : wu) + (size_t)(128 * cw + 32 * r) * DF + 64 * ub + 8 * (c & 7)
                       : role == 2 ? wd + (size_t)(448 * cw + 2 * g) * D + 32 * n6 + 8 * q4
                                   : wo + (size_t)(hw * DH + 16 * (lane >> 3)) * D + 64 * j + 8 * (lane & 7);
  const u64 mA = gld64(ws + W_MODV + (role == 2 ? 3 * D + 32 * n6 + 2 * c : e2) * 4), mB = gld64(ws + W_MODV + (D + e2) * 4), mC = gld64(ws + W_MODV + (2 * D + e2) * 4);
  SCHED();
  float l = 0.f, acc[8];
#pragma unroll
  for (int k = 0; k < 8; ++k) acc[k] = 0.f;
#pragma unroll 1
  for (int f = NRK + cw; f < NRS; f += 4) {
    const float s = bf2f(SC[(f - NRK) * 64]);
    const float p = __builtin_amdgcn_exp2f(fmaf(s, LOG2E, -nref));
    if (!wait_fill(f)) break;
    u32x4 w[16]; read_slot(f, w);
    l += p;
    PV<0, 16, 1, 0>::run(acc, p, w);
  }
  double ad[8], ld = (double)l;
#pragma unroll
  for (int k = 0; k < 8; ++k) ad[k] = (double)acc[k];
#pragma unroll 1
  for (int f = NRS;; ++f) {
    if (!wait_fill(f)) break;
    const LAS u32x4* src = (const LAS u32x4*)(L + (f & 7) * 16384 + cw * 2048) + lane;
    u32x4 k0 = src[0], k1 = src[64], v0 = src[512], v1 = src[576];
    LDSW();
    C[C_DONE + cw] = f + 1;
    asm volatile("" : "+v"(k0), "+v"(k1), "+v"(v0), "+v"(v1) :: "memory");
    float xs[2];
    xs[0] = dot2(k0[3], qv[3], dot2(k0[2], qv[2], dot2(k0[1], qv[1], dot2(k0[0], qv[0], 0.f))));
    xs[1] = dot2(k1[3], qv[3], dot2(k1[2], qv[2], dot2(k1[1], qv[1], dot2(k1[0], qv[0], 0.f))));
    TR_STEP(1, ROR8, 8)
    float d = xs[0]; d += dpp<XOR7>(d); d += dpp<XOR2>(d); d += dpp<XOR1>(d);
    const float t0 = rbf(rbf(d) * SCALE) * LOG2E, t = t0 < -2.0e6f ? -2.0e6f : t0, ti = floorf(t);
    if (__any(t > nref)) {
      const float nn = ceilf(wave_max(t)); const int sh = (int)fmaxf(nref - nn, -4000.f);
      ld = ldexp(ld, sh); nref = nn;
#pragma unroll
      for (int k = 0; k < 8; ++k) ad[k] = ldexp(ad[k], sh);
    }
    const double p = ldexp((double)__builtin_amdgcn_exp2f(t - ti), (int)fmaxf(ti - nref, -4000.f));
    ld = fma(0.125, p, ld);
    pv_row64<0>(ad, p, v0); pv_row64<8>(ad, p, v1);
    if (f + 1 == total_seen) break;
  }
#undef TR_STEP
  const unsigned Tlo = uni(C[C_T]), Thi = uni(C[C_T + 1]);
  const __amdgpu_buffer_rsrc_t rw = __builtin_amdgcn_make_buffer_rsrc((void*)ws, 0, (int)W_USED, 0x00020000);
  {
    LAS unsigned char* M = L + L_MRG + cw * 1040;
    const double n0 = sw32d(ad[0], ad[4]), n1 = sw32d(ad[1], ad[5]), n2 = sw32d(ad[2], ad[6]), n3 = sw32d(ad[3], ad[7]);
    *(LAS f64x2*)(M + 16 + (8 * c + sub2) * 8) = f64x2{sw16d(n0, n2), sw16d(n1, n3)};
    ld += dppd<ROR8>(ld); ld += dppd<ROR4>(ld); ld += dppd<XOR2>(ld); ld += dppd<XOR1>(ld); ld = sw16d(ld, ld); ld = sw32d(ld, ld);
    if (lane == 0) *(LAS f64x2*)M = f64x2{(double)nref, ld};
    LDSW();
    __builtin_amdgcn_s_barrier();
    CFENCE();
    if (cw == 0) {
      f64x2 nl[4], o[4];
#pragma unroll
      for (int w = 0; w < 4; ++w) { nl[w] = *(const LAS f64x2*)(L + L_MRG + w * 1040); o[w] = *(const LAS f64x2*)(L + L_MRG + w * 1040 + 16 + lane * 16); }
      const float Nx = fmaxf(fmaxf((float)nl[0][0], (float)nl[1][0]), fmaxf((float)nl[2][0], (float)nl[3][0]));
      double Ls = 0.0, o0 = 0.0, o1 = 0.0;
#pragma unroll
      for (int w = 0; w < 4; ++w) { const int sh = (int)fmaxf((float)nl[w][0] - Nx, -4000.f); Ls += ldexp(nl[w][1], sh); o0 += ldexp(o[w][0], sh); o1 += ldexp(o[w][1], sh); }
      __builtin_amdgcn_raw_buffer_store_b128(u32x4{dlo(o0) ^ ((dlo(o0) ^ dhi(o0)) == Tlo), dhi(o0), dlo(o1) ^ ((dlo(o1) ^ dhi(o1)) == Thi), dhi(o1)}, rw,
                                             (int)W_R1 + b * 1024 + lane * 16, 0, 16);
      if (lane == 0) __builtin_amdgcn_raw_buffer_store_b128(u32x4{canon(Nx), 0u, canonu(dlo(Ls)), canonu(dhi(Ls))}, rw, (int)W_ML + b * 16, 0, 16);
    }
  }
  if (role == 3) {
    u32x4 wr[16];
#pragma unroll
    for (int i = 0; i < 16; ++i) wr[i] = __builtin_nontemporal_load((const u32x4*)(wp_ + (size_t)i * D));
    u32x4 rec[8], ml; Spin sp;
    auto poll = [&]() {
      CFENCE();
      ml = __builtin_amdgcn_raw_buffer_load_b128(rw, (int)W_ML + hw * 128 + (lane & 7) * 16, 0, 16);
#pragma unroll
      for (int k = 0; k < 8; ++k) rec[k] = __builtin_amdgcn_raw_buffer_load_b128(rw, (int)W_R1 + hw * 8192 + lane * 16 + (k & 3) * 1024, (k >> 2) * 4096, 16);
      SCHED(); };
    auto ready = [&]() -> bool {
      unsigned mn = ~max(max(ml[0], ml[1]), max(ml[2], ml[3]));
#pragma unroll
      for (int k = 0; k < 8; ++k) mn = min(mn, min(rec[k][0] ^ rec[k][1] ^ Tlo, rec[k][2] ^ rec[k][3] ^ Thi));
      return __all(mn != 0u); };
    poll();
    pair_rows<16>(wr);
    while (!ready() && !sp.out()) poll();
    const float ns = __uint_as_float(ml[0]);
    float Nx = fmaxf(ns, dpp<XOR7>(ns)); Nx = fmaxf(Nx, dpp<XOR2>(Nx)); Nx = fmaxf(Nx, dpp<XOR1>(Nx));
    const int sh = (int)fmaxf(ns - Nx, -4000.f);
    double Ls = ldexp(mkd(ml[2], ml[3]), sh); Ls += dppd<XOR7>(Ls); Ls += dppd<XOR2>(Ls); Ls += dppd<XOR1>(Ls);
    double o0 = 0.0, o1 = 0.0;
#define CMB(k) { const int sk = (int)dppu<BC0 + k>((unsigned)sh); o0 += ldexp(mkd(rec[k][0], rec[k][1]), sk); o1 += ldexp(mkd(rec[k][2], rec[k][3]), sk); }
    CMB(0) CMB(1) CMB(2) CMB(3) CMB(4) CMB(5) CMB(6) CMB(7)
#undef CMB
    const float il = __builtin_amdgcn_rcpf((float)Ls);
    const unsigned op = pk2((float)o0 * il, (float)o1 * il);
    float v[8];
#pragma unroll
    for (int k = 0; k < 8; ++k) v[k] = 0.f;
    GV<0, 8, 1, 16>::run(v, &op, wr);
    f32x2 y = red8to2(v);
    y[0] += dpp<ROR8>(y[0]); y[1] += dpp<ROR8>(y[1]);
    LAS f32x2* R = (LAS f32x2*)(L + L_RED1);
    if (c < 8) R[cw * 32 + 4 * c + (sub2 >> 1)] = y;
    LDSW();
    __builtin_amdgcn_s_barrier();
    CFENCE();
    if (cw == 0 && lane < 32) { const f32x2 t = (R[lane] + R[32 + lane]) + (R[64 + lane] + R[96 + lane]);
      gst64(ws + W_WOP + hg * 2048 + j * 256 + lane * 8, mk64(canon(t[0]), canon(t[1]))); }
  } else if (role == 1) {
    u32x4 wr[32];
#pragma unroll
    for (int i = 0; i < 32; ++i) wr[i] = __builtin_nontemporal_load((const u32x4*)(wp_ + (size_t)i * DF));
    u64 pw[8]; Spin sp;
    const unsigned char* src = ws + W_WOP + (128 * cw + 2 * lane) * 4;
    auto poll = [&]() {
#pragma unroll
      for (int k = 0; k < 8; ++k) pw[k] = gld64(src + k * 2048);
      SCHED(); };
    auto ready = [&]() -> bool {
      unsigned mx = 0;
#pragma unroll
      for (int k = 0; k < 8; ++k) mx = max(mx, max((unsigned)pw[k], (unsigned)(pw[k] >> 32)));
      return __all(mx != SENT); };
    poll();
    pair_rows<32>(wr);
    while (!ready() && !sp.out()) poll();
#define PW(k) __builtin_bit_cast(f32x2, pw[k])
    const f32x2 at = ((PW(0) + PW(1)) + (PW(2) + PW(3))) + ((PW(4) + PW(5)) + (PW(6) + PW(7)));
#undef PW
    const float x0 = rbf(lo16(xp) + rbf(__uint_as_float((unsigned)mA) * rbf(at[0]))), x1v = rbf(hi16(xp) + rbf(__uint_as_float((unsigned)(mA >> 32)) * rbf(at[1])));
    if (ub == 0) gst32(ws + W_X1 + (64 * cw + lane) * 4, canonu(pk2(x0, x1v)));
    const float piv = __uint_as_float(uni(__float_as_uint(x0)));
    const float d0 = x0 - piv, d1 = x1v - piv;
    const float s1 = wave_sum(d0 + d1), s2 = wave_sum(fmaf(d0, d0, d1 * d1));
    const float mw = fmaf(s1, 1.f / 128.f, piv), m2 = fmaf(-s1 * (1.f / 128.f), s1, s2);
    LAS float* R = (LAS float*)(L + L_RED1);
    if (lane == 0) { f32x2 t = {mw, m2}; *(LAS f32x2*)(R + 2 * cw) = t; }
    LDSW();
    __builtin_amdgcn_s_barrier();
    CFENCE();
    f32x2 st[4];
#pragma unroll
    for (int w = 0; w < 4; ++w) st[w] = *(const LAS f32x2*)(R + 2 * w);
    const float mean = ((st[0][0] + st[1][0]) + (st[2][0] + st[3][0])) * 0.25f;
    float M2 = (st[0][1] + st[1][1]) + (st[2][1] + st[3][1]);
#pragma unroll
    for (int w = 0; w < 4; ++w) { const float dm = st[w][0] - mean; M2 = fmaf(128.f * dm, dm, M2); }
    const float den = ln_den(M2 * (1.f / D)), mb = rbf(mean);
    const unsigned hp = pk2(modulate(x0, mb, den, __uint_as_float((unsigned)mC), __uint_as_float((unsigned)mB)),
                            modulate(x1v, mb, den, __uint_as_float((unsigned)(mC >> 32)), __uint_as_float((unsigned)(mB >> 32))));
    float v[8];
#pragma unroll
    for (int k = 0; k < 8; ++k) v[k] = 0.f;
    GV<0, 16, 0, 32>::run(v, &hp, wr);
    LAS float* R2 = (LAS float*)(L + L_RED2);
    *(LAS f32x2*)(R2 + cw * 128 + 8 * c + sub2) = red8to2(v);
    LDSW();
    __builtin_amdgcn_s_barrier();
    CFENCE();
    if (cw == 0) {
      const float gg = rbf((R2[lane] + R2[128 + lane]) + (R2[256 + lane] + R2[384 + lane]));
      const float uu = rbf((R2[64 + lane] + R2[192 + lane]) + (R2[320 + lane] + R2[448 + lane]));
      const float z = 0.7978845608028654f * fmaf(0.044715f * gg * gg, gg, gg);
      const float ge = rbf(gg * __builtin_amdgcn_rcpf(1.f + __builtin_amdgcn_exp2f(-2.f * LOG2E * z)));
      const unsigned hb = __float_as_uint(rbf(ge * uu));
      const unsigned pr = phi(hb, dppu<XOR1>(hb));
      if ((lane & 1) == 0) gst32(ws + W_H2 + ub * 128 + (lane >> 1) * 4, canonu(pr));
    }
  } else if (role == 2) {
    u32x4 wr[28];
#pragma unroll
    for (int i = 0; i < 28; ++i) wr[i] = __builtin_nontemporal_load((const u32x4*)(wp_ + (size_t)((i >> 1) * 32 + (i & 1)) * D));
    unsigned hp[4], xa; Spin sp;
    const unsigned char* src = ws + W_H2 + (224 * cw + 16 * q4 + g) * 4;
    auto poll = [&]() {
      xa = gld32(ws + W_X1 + (16 * n6 + c) * 4);
      hp[0] = gld32(src); hp[1] = gld32(src + 256); hp[2] = gld32(src + 512); hp[3] = gld32(src + (q4 < 2 ? 768 : 0));
      SCHED(); };
    poll();
    pair_rows<28>(wr);
    while (!__all(max(max(max(hp[0], hp[1]), max(hp[2], hp[3])), xa) != SENT) && !sp.out()) poll();
    float v[8];
#pragma unroll
    for (int k = 0; k < 8; ++k) v[k] = 0.f;
    GV<0, 14, 2, 28>::run(v, hp, wr);
    f32x2 y = red8to2(v);
    y[0] += dpp<ROR8>(y[0]); y[1] += dpp<ROR8>(y[1]); y[0] += dpp<ROR4>(y[0]); y[1] += dpp<ROR4>(y[1]);
    LAS f32x2* R = (LAS f32x2*)(L + L_RED2);
    if (c < 4) R[cw * 16 + 4 * q4 + (sub2 >> 1)] = y;
    LDSW();
    __builtin_amdgcn_s_barrier();
    CFENCE();
    if (cw == 0) {
      const f32x2 dsum = (R[c] + R[16 + c]) + (R[32 + c] + R[48 + c]);
      const float o0 = lo16(xa) + rbf(__uint_as_float((unsigned)mA) * rbf(dsum[0]));
      const float o1 = hi16(xa) + rbf(__uint_as_float((unsigned)(mA >> 32)) * rbf(dsum[1]));
      if (lane < 16) *(unsigned*)(out + 32 * n6 + 2 * c) = pk2(o0, o1);
      if (n6 == 0 && lane == 0) gst64(ws + W_EPOCH, mk64(Tlo, Thi));
    }
  }
}

extern "C" void kernel_launch(void* const* d_in, const int* in_sizes, int n_in, void* d_out, int out_size, void* d_ws, size_t ws_size, hipStream_t stream) {
  (void)hipFuncSetAttribute((const void*)fused_decode, hipFuncAttributeMaxDynamicSharedMemorySize, L_END);
  fused_decode<<<NWG, NTHR, L_END, stream>>>((const u16*)d_in[0], (const u16*)d_in[1], (const u16*)d_in[2], (const u16*)d_in[3], (const u16*)d_in[4],
                                             (const u16*)d_in[6], (const u16*)d_in[7], (const u16*)d_in[8], (const u16*)d_in[9], (const u16*)d_in[10],
                                             (u16*)d_out, (unsigned char*)d_ws);
}
```
